```python
import torch

def _softmax(x, axis=-1):
    return torch.softmax(x, dim=axis)
def _gelu(x):
    return torch.nn.functional.gelu(x, approximate="tanh")
def _silu(x):
    return torch.nn.functional.silu(x)
def _sigmoid(x):
    return torch.sigmoid(x)
def _layernorm(x, axis=-1, eps=1e-5):
    m = x.mean(dim=axis, keepdim=True)
    v = x.var(dim=axis, keepdim=True, unbiased=False)
    return (x - m) / torch.sqrt(v + eps)
def _scatter_add(tgt, idx, src, axis=0):
    return tgt.clone().index_add_(axis, idx, src)
def _segment_sum(src, seg, n):
    out = torch.zeros((n,) + src.shape[1:], dtype=src.dtype)
    return out.index_add_(0, seg, src)
def _pad(x, pw):
    flat = tuple(v for pair in reversed(pw) for v in pair)
    return torch.nn.functional.pad(x, flat)

kernel_name = 'parallel_sum__mha_block__swiglu_ffn__prompt'

def setup_inputs():

    _w = (lambda a: (lambda t: t.to(torch.bfloat16) if t.is_floating_point() else t)(torch.as_tensor(a)))
    def _A_setup():
        B, S, D, H = 1, 2048, 1024, 32
        return {
            "x": _w((lambda *s: torch.randn(*s, dtype=torch.bfloat16) * 0.05)(B, S, D)),
            "wqkv": _w((lambda *s: torch.randn(*s, dtype=torch.bfloat16) * 0.05)(D, 3 * D)),
            "wo": _w((lambda *s: torch.randn(*s, dtype=torch.bfloat16) * 0.05)(D, D)),
        }
    _A_in = _A_setup()

    def _B_setup():
        B, S, D, F = 1, 2048, 1024, 3584
        return {
            "x": _w((lambda *s: torch.randn(*s, dtype=torch.bfloat16) * 0.05)(B, S, D)),
            "w_gate": _w((lambda *s: torch.randn(*s, dtype=torch.bfloat16) * 0.05)(D, F)),
            "w_up": _w((lambda *s: torch.randn(*s, dtype=torch.bfloat16) * 0.05)(D, F)),
            "w_down": _w((lambda *s: torch.randn(*s, dtype=torch.bfloat16) * 0.05)(F, D)),
        }
    _B_in = _B_setup()

    return {
        "A_x": _A_in["x"],
        "A_wqkv": _A_in["wqkv"],
        "A_wo": _A_in["wo"],
        "B_x": _B_in["x"],
        "B_w_gate": _B_in["w_gate"],
        "B_w_up": _B_in["w_up"],
        "B_w_down": _B_in["w_down"],
    }

def reference(A_x, A_wqkv, A_wo, B_x, B_w_gate, B_w_up, B_w_down):
    def _A_ref(x, wqkv, wo):
        B, S, D = x.shape
        H = 32
        Dh = D // H
        qkv = (lambda x, *d: x.permute(*d))((x @ wqkv).reshape(B, S, 3, H, Dh), 2, 0, 3, 1, 4)
        q, k, v = qkv[0], qkv[1], qkv[2]
        scores = torch.einsum("bhqd,bhkd->bhqk", q, k) * (Dh ** -0.5)
        p = _softmax(scores, axis=-1)
        o = torch.einsum("bhqk,bhkd->bhqd", p, v)
        return (lambda x, *d: x.permute(*d))(o, 0, 2, 1, 3).reshape(B, S, D) @ wo

    def _B_ref(x, w_gate, w_up, w_down):
        g = _silu(x @ w_gate)
        u = x @ w_up
        return (g * u) @ w_down

    x = A_x
    xn = _layernorm(x, axis=-1)
    return x + _A_ref(xn, A_wqkv, A_wo) + _B_ref(xn, B_w_gate, B_w_up, B_w_down)
```

```cpp
#include <hip/hip_runtime.h>
#include <stdint.h>
#include <type_traits>

typedef unsigned short u16;
typedef short bf16x8 __attribute__((ext_vector_type(8)));
typedef short s16x4 __attribute__((ext_vector_type(4)));
typedef float f32x2 __attribute__((ext_vector_type(2)));
typedef float f32x4 __attribute__((ext_vector_type(4)));
typedef float f32x16 __attribute__((ext_vector_type(16)));
typedef unsigned u32x2 __attribute__((ext_vector_type(2)));
typedef unsigned u32x4 __attribute__((ext_vector_type(4)));
typedef __bf16 bf16x2_t __attribute__((ext_vector_type(2)));
#define LAS __attribute__((address_space(3)))
#define DI __device__ __forceinline__
#define STAMP(kid, i) do {} while (0)
#define STAMPR(kid, i) do {} while (0)

#define LN_PF 4
constexpr int S_ = 2048, D_ = 1024, H_ = 32, F_ = 3584;
constexpr int NKC1 = D_ / 32;
constexpr int K2 = D_ + F_;
constexpr int NKC2 = K2 / 32;

constexpr size_t WS_XN = 0;
constexpr size_t WS_Q = WS_XN + (size_t)S_ * D_ * 2;
constexpr size_t WS_K = WS_Q + (size_t)S_ * D_ * 2;
constexpr size_t WS_V = WS_K + (size_t)S_ * D_ * 2;
constexpr size_t WS_A2 = WS_V + (size_t)S_ * D_ * 2;
constexpr size_t WS_CNT = WS_A2 + (size_t)S_ * K2 * 2;
constexpr size_t WS_PART = WS_CNT + 65536;

DI unsigned cvtpk(float lo, float hi) { f32x2 v = {lo, hi}; bf16x2_t b = __builtin_convertvector(v, bf16x2_t); return __builtin_bit_cast(unsigned, b); }
DI float bf2f(u16 v) { return __uint_as_float((unsigned)v << 16); }
DI float rbf(float v) { return __uint_as_float(cvtpk(v, v) << 16); }
DI void glds16(const void* g, unsigned lds_dst_) {
    unsigned keep; const unsigned lds_dst = __builtin_amdgcn_readfirstlane(lds_dst_);
    asm volatile("s_mov_b32 %0, m0\n\ts_mov_b32 m0, %2\n\ts_nop 0\n\tglobal_load_lds_dwordx4 %1, off\n\ts_mov_b32 m0, %0" : "=&s"(keep) : "v"(g), "s"(lds_dst) : "memory");
}
DI void glds16s(const void* sbase, unsigned voff, unsigned lds_dst_) {
    unsigned keep; const unsigned lds_dst = __builtin_amdgcn_readfirstlane(lds_dst_);
    asm volatile("s_mov_b32 %0, m0\n\ts_mov_b32 m0, %2\n\ts_nop 0\n\tglobal_load_lds_dwordx4 %1, %3\n\ts_mov_b32 m0, %0" : "=&s"(keep) : "v"(voff), "s"(lds_dst), "s"(sbase) : "memory");
}
DI void glds16s_dev(const void* sbase, unsigned voff, unsigned lds_dst_) {
    unsigned keep; const unsigned lds_dst = __builtin_amdgcn_readfirstlane(lds_dst_);
    asm volatile("s_mov_b32 %0, m0\n\ts_mov_b32 m0, %2\n\ts_nop 0\n\tglobal_load_lds_dwordx4 %1, %3 sc1\n\ts_mov_b32 m0, %0" : "=&s"(keep) : "v"(voff), "s"(lds_dst), "s"(sbase) : "memory");
}
DI void gtouch(const void* g, unsigned lds_dst_) {
    unsigned keep; const unsigned lds_dst = __builtin_amdgcn_readfirstlane(lds_dst_);
    asm volatile("s_mov_b32 %0, m0\n\ts_mov_b32 m0, %2\n\ts_nop 0\n\tglobal_load_lds_dword %1, off\n\ts_mov_b32 m0, %0" : "=&s"(keep) : "v"(g), "s"(lds_dst) : "memory");
}
template <int N> DI void wait_vm() { asm volatile("s_waitcnt vmcnt(%0)" ::"n"(N) : "memory"); }
DI void wait_lgkm0() { asm volatile("s_waitcnt lgkmcnt(0)" ::: "memory"); }
#define SB() __builtin_amdgcn_sched_barrier(0)
#define PIN(x) asm volatile("" : "+v"(x))
DI void bar() { SB(); asm volatile("s_barrier" ::: "memory"); SB(); }
DI int lane_id() { int l; asm volatile("v_mbcnt_lo_u32_b32 %0, -1, 0\n\tv_mbcnt_hi_u32_b32 %0, -1, %0" : "=v"(l)); return l; }
DI s16x4 trread(const LAS char* p) { return __builtin_bit_cast(s16x4, __builtin_amdgcn_ds_read_tr16_b64_v4i16((LAS s16x4*)p)); }
#define MFMA32(a, b, c) __builtin_amdgcn_mfma_f32_32x32x16_bf16(a, b, c, 0, 0, 0)
#define MFMA16(a, b, c) __builtin_amdgcn_mfma_f32_16x16x32_bf16(a, b, c, 0, 0, 0)

__global__ __launch_bounds__(256) void ln_kernel(const u16* __restrict__ x, u16* __restrict__ xn, unsigned* __restrict__ cnt, const u16* __restrict__ wqkv, const u16* __restrict__ wg, const u16* __restrict__ wu) {
    if (blockIdx.x == 0 && threadIdx.x < 136) cnt[threadIdx.x * 64] = 0u;
    const int lane = threadIdx.x & 63, w = threadIdx.x >> 6;
    const int row = blockIdx.x * 4 + w;
    const u32x4* src = (const u32x4*)(x + (size_t)row * D_ + lane * 16);
    u32x4 a = src[0], b = src[1];
    {
        const unsigned t = blockIdx.x * 256 + threadIdx.x; unsigned d0 = 0, d1 = 0, d2 = 0;
        if (t < (unsigned)(D_ * F_ * 2 / 128) * LN_PF / 16) {
            asm volatile("global_load_dword %0, %1, off" : "+v"(d0) : "v"((const char*)wg + (size_t)t * 128) : "memory");
            asm volatile("global_load_dword %0, %1, off" : "+v"(d1) : "v"((const char*)wu + (size_t)t * 128) : "memory"); }
        if (t < (unsigned)(D_ * 3072 * 2 / 128) * LN_PF / 16) asm volatile("global_load_dword %0, %1, off" : "+v"(d2) : "v"((const char*)wqkv + (size_t)t * 128) : "memory");
        asm volatile("s_waitcnt vmcnt(0)" : "+v"(d0), "+v"(d1), "+v"(d2) :: "memory");
    }
    float v[16];
#pragma unroll
    for (int i = 0; i < 4; ++i) { v[2 * i] = __uint_as_float(a[i] << 16); v[2 * i + 1] = __uint_as_float(a[i] & 0xffff0000u);
                                  v[8 + 2 * i] = __uint_as_float(b[i] << 16); v[8 + 2 * i + 1] = __uint_as_float(b[i] & 0xffff0000u); }
    float s = 0.f;
#pragma unroll
    for (int i = 0; i < 16; ++i) s += v[i];
#pragma unroll
    for (int o = 32; o >= 1; o >>= 1) s += __shfl_xor(s, o);
    const float mean = s * (1.0f / D_);
    float q = 0.f;
#pragma unroll
    for (int i = 0; i < 16; ++i) { const float d = v[i] - mean; q += d * d; }
#pragma unroll
    for (int o = 32; o >= 1; o >>= 1) q += __shfl_xor(q, o);
    const float var = q * (1.0f / D_);
    const float rs = 1.0f / sqrtf(var + 1e-5f);
    float o[16];
#pragma unroll
    for (int i = 0; i < 16; ++i) o[i] = (v[i] - mean) * rs;
    u32x4 w0 = {cvtpk(o[0], o[1]), cvtpk(o[2], o[3]), cvtpk(o[4], o[5]), cvtpk(o[6], o[7])};
    u32x4 w1 = {cvtpk(o[8], o[9]), cvtpk(o[10], o[11]), cvtpk(o[12], o[13]), cvtpk(o[14], o[15])};
    const int mt = row >> 4, c = row & 15, kc = lane >> 1, g = (lane & 1) * 2;
    u32x4* dst = (u32x4*)xn + ((size_t)(mt * NKC1 + kc) * 64 + g * 16 + c);
    dst[0] = w0; dst[16] = w1;
}

template <int NLS, int NLV, int NDEV = 0> struct Stage { const char* sp[NLS]; unsigned vo[NLS], sinc[NLS], sl[NLS]; const char* gp[NLV ? NLV : 1]; unsigned ginc[NLV ? NLV : 1], gl[NLV ? NLV : 1]; };
template <int NLS, int NLV, int NDEV> DI void stage_issue(const Stage<NLS, NLV, NDEV>& S, int i, unsigned base) {
    if (i < NDEV) glds16s_dev(S.sp[i], S.vo[i], base + S.sl[i]); else if (i < NLS) glds16s(S.sp[i], S.vo[i], base + S.sl[i]); else glds16(S.gp[i - NLS], base + S.gl[i - NLS]); }
struct NoHook { static constexpr int JP = -1, NPRO = 0, NX = 0; DI void extra(unsigned) {} DI void prologue() const {} DI void first() const {} DI void pre() {} DI void post() {} template <class St> DI void jump(St&) {} };
template <bool B> using BoolC = std::integral_constant<bool, B>;
template <int N> using IntC = std::integral_constant<int, N>;
template <int MT, int NT, int N0, int CPP, int NS, int NPH, int CHB, int NLS, int NLV, int NDEV, class Hook>
DI void gemm_core(f32x4 (&acc)[MT][NT], Stage<NLS, NLV, NDEV>& S, const unsigned lds0, unsigned xrd, unsigned (&wrd)[4], const bool trailing, Hook& H) {
    constexpr int SLOTB = CPP * CHB, NLD = NLS + NLV, NRD = CPP * (MT + NT);
    static_assert(NS == 3 || NS == 4, "ring depth");
#pragma unroll
    for (int p = 0; p < NS - 1; ++p) {
#pragma unroll
        for (int i = 0; i < NLD; ++i) stage_issue(S, i, lds0 + p * SLOTB);
#pragma unroll
        for (int i = 0; i < NLS; ++i) S.sp[i] += S.sinc[i];
#pragma unroll
        for (int i = 0; i < NLV; ++i) S.gp[i] += S.ginc[i];
    }
    H.prologue();
#pragma unroll
    for (int m = 0; m < MT; ++m)
#pragma unroll
        for (int n = 0; n < NT; ++n) PIN(acc[m][n]);
    wait_vm<(NS - 2) * NLD + Hook::NPRO>();
    bar();
    H.first();
    if (trailing) bar();
    STAMP(MT * NT == 40 ? 0 : 2, 1);
    int slot = 0, pslot = (NS - 1) * SLOTB;
    auto phase = [&](auto issue_c, auto wait_c, auto hook_c, auto extra_c) {
        constexpr bool ISSUE = decltype(issue_c)::value, HOOK = decltype(hook_c)::value, EXTRA = decltype(extra_c)::value; constexpr int WAITN = decltype(wait_c)::value;
        bf16x8 xf[CPP][MT], wf[CPP][NT];
        if (HOOK) H.pre();
#pragma unroll
        for (int r = 0; r < NRD; ++r) {
            if (ISSUE) {
#pragma unroll
                for (int i = 0; i < NLD; ++i) if (r == (i * NRD) / NLD + 1) stage_issue(S, i, lds0 + pslot);
            }
            const int c = r / (MT + NT), k = r % (MT + NT);
            if (k < MT) xf[c][k] = *(const LAS bf16x8*)(uintptr_t)(xrd + c * CHB + k * 1024);
            else { const int n = k - MT; const LAS char* p = (const LAS char*)(uintptr_t)(wrd[(N0 + n) & 3] + c * CHB + ((N0 + n) >> 2) * 4096);
                const s16x4 lo = trread(p), hi = trread(p + 512); wf[c][n] = __builtin_shufflevector(lo, hi, 0, 1, 2, 3, 4, 5, 6, 7); }
        }
        if (EXTRA) H.extra(S.vo[0]);
        pslot = slot; slot += SLOTB; int dl = SLOTB; if (slot == NS * SLOTB) { slot = 0; dl = -(NS - 1) * SLOTB; }
        SB();
#pragma unroll
        for (int i = 0; i < 4; ++i) { wrd[i] += dl; PIN(wrd[i]); }
        xrd += dl; PIN(xrd);
        if (ISSUE) {
#pragma unroll
            for (int i = 0; i < NLS; ++i) S.sp[i] += S.sinc[i];
#pragma unroll
            for (int i = 0; i < NLV; ++i) { S.gp[i] += S.ginc[i]; PIN(S.gp[i]); }
        }
        if (HOOK) H.jump(S);
        SB();
        wait_vm<WAITN>();
        if (HOOK) H.post();
        wait_lgkm0();
        bar();
        __builtin_amdgcn_s_setprio(1);
#pragma unroll
        for (int c = 0; c < CPP; ++c)
#pragma unroll
            for (int m = 0; m < MT; ++m)
#pragma unroll
                for (int n = 0; n < NT; ++n) acc[m][n] = MFMA16(wf[c][n], xf[c][m], acc[m][n]);
        __builtin_amdgcn_s_setprio(0);
        bar();
    };
    constexpr int NI = NPH - (NS - 1), JP = Hook::JP, NX = Hook::NX;
    constexpr int WN = (NS - 2) * NLD, WX = WN + (NX ? 1 : 0);
    if constexpr (JP >= 0) {
#pragma unroll 1
        for (int j = 0; j < JP; ++j) phase(BoolC<true>{}, IntC<WX>{}, BoolC<false>{}, BoolC<(NX > 0)>{});
        phase(BoolC<true>{}, IntC<WX>{}, BoolC<true>{}, BoolC<(NX > 0)>{});
    }
    if constexpr (NX > 0) {
#pragma unroll 1
        for (int j = JP + 1; j < NX; ++j) phase(BoolC<true>{}, IntC<WX>{}, BoolC<false>{}, BoolC<true>{});
    }
#pragma unroll 1
    for (int j = (NX > 0 ? NX : JP + 1); j < NI; ++j) phase(BoolC<true>{}, IntC<WN>{}, BoolC<false>{}, BoolC<false>{});
    if (NS == 4) phase(BoolC<false>{}, IntC<NLD>{}, BoolC<false>{}, BoolC<false>{});
#pragma unroll 1
    for (int j = 0; j < 2; ++j) phase(BoolC<false>{}, IntC<0>{}, BoolC<false>{}, BoolC<false>{});
}
DI int wswz(int r) { return ((r >> 1) & 1) | (((r >> 3) & 1) << 1); }

typedef __attribute__((address_space(1))) unsigned gu32;
typedef __attribute__((address_space(1))) unsigned long long gu64;
#define RLX_AGENT __ATOMIC_RELAXED, __HIP_MEMORY_SCOPE_AGENT
DI void st8_dev(void* p, u32x2 v) { __hip_atomic_store((gu64*)p, __builtin_bit_cast(unsigned long long, v), RLX_AGENT); }
DI void signal(unsigned* c) { __hip_atomic_fetch_add((gu32*)c, 1u, RLX_AGENT); }
DI void wait_ge(unsigned* c, unsigned n) {
#pragma unroll 1
    for (unsigned spins = 0; spins < (1u << 22); ++spins) {
        if ((unsigned)__builtin_amdgcn_readfirstlane(__hip_atomic_load((gu32*)c, RLX_AGENT)) >= n) break;
        __builtin_amdgcn_s_sleep(1);
    }
}
constexpr int CNT_QKV = 64, CNT_H = 96, CNT_O = 104, CNT_N = 136;

constexpr int G1_XB = 16 * 1024, G1_CHB = G1_XB + 5 * 4096, G1_NS = 4;
constexpr float QSCALE = 0.17677669529663687f * 1.4426950408889634f;
struct G1Hook : NoHook {
    static constexpr int JP = 7, NX = 12;
    const char *xp, *xp2; unsigned xinc, xinc2, dummy;
    DI void init(const u16* wqkv, const u16* wg, const u16* wu, const u16* wo, const u16* wd, unsigned gw, unsigned dummy_) {
        static_assert(LN_PF == 4, "rows [0,256) of wqkv / gate / up are requested by the LayerNorm kernel");
        dummy = dummy_;
        if (gw < 672) { xp = (const char*)wg + (size_t)256 * F_ * 2 + gw * 1024; xinc = 672 * 1024; }
        else if (gw < 1344) { xp = (const char*)wu + (size_t)256 * F_ * 2 + (gw - 672) * 1024; xinc = 672 * 1024; }
        else if (gw < 1920) { xp = (const char*)wqkv + (size_t)256 * 3072 * 2 + (gw - 1344) * 1024; xinc = 576 * 1024; }
        else { xp = (const char*)wo + (gw - 1920) * 1024; xinc = 128 * 1024; }
        if (gw < 1792) { xp2 = (const char*)wd + gw * 1024; xinc2 = 1792 * 1024; }
        else { xp2 = (const char*)wo + 1024 * 1024 + (gw - 1792) * 1024; xinc2 = 256 * 1024; }
    }
    DI void extra(unsigned vo) { glds16s(xp, vo, dummy); xp += xinc; }
    template <class St> DI void jump(St&) { xp = xp2; xinc = xinc2; }
};
template <int GRP>
DI void gemm1_body(f32x4 (&acc)[4][10], const u16* xn, const u16* wqkv, const u16* wg, const u16* wu, const u16* wo, const u16* wd, unsigned lds0, int bm, int bn, int w) {
    constexpr int NLV = GRP == 0 ? 3 : 2;
    const int lane = lane_id();
    Stage<2, NLV> S;
#pragma unroll
    for (int i = 0; i < 2; ++i) { const int f = 2 * w + i; S.sp[i] = (const char*)xn + ((size_t)(bm * 16 + f) * NKC1) * 1024; S.vo[i] = lane * 16; S.sinc[i] = 1024; S.sl[i] = f * 1024; }
#pragma unroll
    for (int i = 0; i < NLV; ++i) {
        const int b = w + 8 * i, P = bn * 5 + (b >> 2), r = 8 * (b & 3) + (lane >> 3), cp = lane & 7, j = (cp >> 1) ^ wswz(r), half = cp & 1;
        if (P < 48) { S.gp[i] = (const char*)(wqkv + (size_t)r * 3072 + 64 * P + 16 * j + 8 * half); S.ginc[i] = 32 * 3072 * 2; }
        else { S.gp[i] = (const char*)(((j & 1) ? wu : wg) + (size_t)r * F_ + 32 * (P - 48) + 16 * (j >> 1) + 8 * half); S.ginc[i] = 32 * F_ * 2; }
        S.gl[i] = G1_XB + b * 1024;
    }
    const int g = lane >> 4, q = (lane & 15) >> 2, pp = lane & 3, sw = ((q >> 1) & 1) | ((g & 1) << 1);
    unsigned wrd[4];
#pragma unroll
    for (int j = 0; j < 4; ++j) wrd[j] = lds0 + G1_XB + g * 1024 + q * 128 + 32 * (j ^ sw) + 8 * pp;
    const unsigned xrd = lds0 + (w & 3) * 4 * 1024 + lane * 16;
    G1Hook H; H.init(wqkv, wg, wu, wo, wd, blockIdx.x * 8 + w, lds0 + G1_NS * G1_CHB + w * 1024);
    gemm_core<4, 10, 10 * GRP, 1, G1_NS, NKC1, G1_CHB>(acc, S, lds0, xrd, wrd, GRP == 1, H);
}
DI void phase1(const u16* xn, const u16* wqkv, const u16* wg, const u16* wu, const u16* wo, const u16* wd, u16* Qf, u16* Kf, u16* Vf, u16* a2, LAS char* smem, unsigned lds0, int bm, int bn, int w) {
    f32x4 acc[4][10];
#pragma unroll
    for (int m = 0; m < 4; ++m)
#pragma unroll
        for (int n = 0; n < 10; ++n) acc[m][n] = (f32x4){0.f, 0.f, 0.f, 0.f};
    if (w < 4) gemm1_body<0>(acc, xn, wqkv, wg, wu, wo, wd, lds0, bm, bn, w);
    else gemm1_body<1>(acc, xn, wqkv, wg, wu, wo, wd, lds0, bm, bn, w);
    STAMP(0,2);
    const int lane2 = lane_id();
    const int wm = w & 3, wn = w >> 2, c = lane2 & 15, g = lane2 >> 4;
    const int mt0 = bm * 16 + wm * 4;
#pragma unroll
    for (int i = 0; i < 5; ++i) {
        if (i == 1 && w < 4) bar();
        const int pr = bn * 10 + wn * 5 + i;
        if (pr < 64) {
            const int head = pr & 31; u16* base = pr < 32 ? Qf : Kf; const float sc = pr < 32 ? QSCALE : 1.0f;
            u16* dst = base + ((size_t)(head * 64 + (mt0 >> 1)) * 2 * 64 + c + 32 * (g >> 1)) * 8 + 4 * (g & 1);
#pragma unroll
            for (int m = 0; m < 4; ++m)
#pragma unroll
                for (int dh = 0; dh < 2; ++dh) { const f32x4 v = acc[m][2 * i + dh] * sc; u32x2 o = {cvtpk(v[0], v[1]), cvtpk(v[2], v[3])};
                    st8_dev(dst + (((m >> 1) * 2 + dh) * 64 + (m & 1) * 16) * 8, o); }
        } else if (pr < 96) {
            const int head = pr - 64;
            LAS char* tb = smem + w * 4096;
#pragma unroll
            for (int m = 0; m < 4; ++m)
#pragma unroll
                for (int dh = 0; dh < 2; ++dh) { const f32x4 v = acc[m][2 * i + dh]; u32x2 o = {cvtpk(v[0], v[1]), cvtpk(v[2], v[3])};
                    *(LAS u32x2*)(tb + (m * 2 + dh) * 512 + c * 32 + g * 8) = o; }
            u16* dst = Vf + ((size_t)(head * 64 + (mt0 >> 1)) * 2 * 64 + c + 32 * (g & 1)) * 8 + 4 * (g >> 1);
#pragma unroll
            for (int m = 0; m < 4; ++m)
#pragma unroll
                for (int dh = 0; dh < 2; ++dh) { const s16x4 t = trread(tb + (m * 2 + dh) * 512 + (4 * g + (c >> 2)) * 32 + (c & 3) * 8);
                    st8_dev(dst + (m * 64 + dh * 16) * 8, __builtin_bit_cast(u32x2, t)); }
        } else {
            const int hp = pr - 96;
            u16* dst = a2 + ((size_t)(mt0 * NKC2 + 32 + (hp >> 1)) * 64 + ((hp & 1) * 2 + (g >> 1)) * 16 + c) * 8 + 4 * (g & 1);
#pragma unroll
            for (int m = 0; m < 4; ++m) {
                const f32x4 gt = acc[m][2 * i], up = acc[m][2 * i + 1]; f32x4 hv;
#pragma unroll
                for (int r = 0; r < 4; ++r) hv[r] = gt[r] * up[r] * __builtin_amdgcn_rcpf(1.0f + __builtin_amdgcn_exp2f(-1.4426950408889634f * gt[r]));
                u32x2 o = {cvtpk(hv[0], hv[1]), cvtpk(hv[2], hv[3])};
                st8_dev(dst + (size_t)m * NKC2 * 512, o);
            }
        }
    }
}

#define P3PF_H 7
struct KV { bf16x8 f0, f1; };
DI bf16x8 ldfrag(__amdgpu_buffer_rsrc_t rs, unsigned voff, unsigned soff) { return __builtin_bit_cast(bf16x8, __builtin_amdgcn_raw_buffer_load_b128(rs, voff, soff, 16)); }
DI void touch(const char* p, unsigned& d) { asm volatile("global_load_dword %0, %1, off" : "+v"(d) : "v"(p) : "memory"); }
DI void phase2(const u16* Qf, u16* a2, unsigned* hcnt, float* red, int head, int qb, int w, const u16* wo, const u16* wd, int xcd, int idx) {
    const int lane = lane_id();
    const int kq = w & 3, qg = qb * 2 + (w >> 2);
    const __amdgpu_buffer_rsrc_t rs = __builtin_amdgcn_make_buffer_rsrc((void*)Qf, 0, 3 * S_ * D_ * 2, 0x00020000);
    const unsigned vo = lane * 16;
    const unsigned ko = S_ * D_ * 2 + (head * 64 + kq * 16) * 2048, vv = 2 * S_ * D_ * 2 + (head * 64 + kq * 16) * 2048, qo = (head * 64 + qg * 4) * 2048;
    KV kA, kB, vA, vB; bf16x8 q[4][2];
    kA.f0 = ldfrag(rs, vo, ko); kA.f1 = ldfrag(rs, vo + 1024, ko);
#pragma unroll
    for (int i = 0; i < 4; ++i) { q[i][0] = ldfrag(rs, vo, qo + i * 2048); q[i][1] = ldfrag(rs, vo + 1024, qo + i * 2048); }
    vA.f0 = ldfrag(rs, vo, vv); vA.f1 = ldfrag(rs, vo + 1024, vv); kB.f0 = ldfrag(rs, vo, ko + 2048); kB.f1 = ldfrag(rs, vo + 1024, ko + 2048);
    vB.f0 = (bf16x8){0, 0, 0, 0, 0, 0, 0, 0}; vB.f1 = vB.f0;
    bf16x8 sel;
    { const short one = ((lane & 15) == ((lane >> 4) & 1)) ? (short)0x3F80 : (short)0;
#pragma unroll
      for (int i = 0; i < 8; ++i) sel[i] = one; }
    f32x16 O[4], sA, sB, Z; f32x4 L[4];
#pragma unroll
    for (int i = 0; i < 16; ++i) { Z[i] = 0.f; O[0][i] = 0.f; O[1][i] = 0.f; O[2][i] = 0.f; O[3][i] = 0.f; }
#pragma unroll
    for (int i = 0; i < 4; ++i) L[i] = (f32x4){0.f, 0.f, 0.f, 0.f};
    u32x4 p0, p1 = {0u, 0u, 0u, 0u};
    sA = MFMA32(kA.f0, q[0][0], Z); sA = MFMA32(kA.f1, q[0][1], sA);
    STAMP(1,1);
#define EX(x) x = __builtin_amdgcn_exp2f(x)
#define BC(x) __builtin_bit_cast(bf16x8, x)
#define STEP(C, N, QI, KN, VC, VP, LD1, LD2) do { constexpr int NQ = ((QI) + 1) & 3, PQ = ((QI) + 3) & 3; \
    N = MFMA32(KN.f0, q[NQ][0], Z); EX(C[0]); EX(C[1]); EX(C[2]); EX(C[3]); PIN(C); SB(); \
    O[PQ] = MFMA32(VP.f1, BC(p1), O[PQ]); LD1; EX(C[4]); EX(C[5]); EX(C[6]); EX(C[7]); PIN(C); SB(); \
    N = MFMA32(KN.f1, q[NQ][1], N); LD2; \
    p0 = (u32x4){cvtpk(C[0], C[1]), cvtpk(C[2], C[3]), cvtpk(C[4], C[5]), cvtpk(C[6], C[7])}; PIN(p0); EX(C[8]); EX(C[9]); PIN(C); SB(); \
    L[PQ] = MFMA16(sel, BC(p1), L[PQ]); EX(C[10]); EX(C[11]); PIN(C); SB(); \
    O[QI] = MFMA32(VC.f0, BC(p0), O[QI]); EX(C[12]); EX(C[13]); EX(C[14]); EX(C[15]); PIN(C); SB(); \
    L[QI] = MFMA16(sel, BC(p0), L[QI]); p1 = (u32x4){cvtpk(C[8], C[9]), cvtpk(C[10], C[11]), cvtpk(C[12], C[13]), cvtpk(C[14], C[15])}; PIN(p1); SB(); \
    } while (0)
#define NOP_ ((void)0)
#define LDKV(X, base, t) (X.f0 = ldfrag(rs, vo, (base) + (t)), X.f1 = ldfrag(rs, vo + 1024, (base) + (t)))
    for (int t = 0; t < 16; t += 2) {
        const unsigned t1 = (t + 1) * 2048, t2 = ((t + 2) & 15) * 2048, t3 = ((t + 3) & 15) * 2048;
        STEP(sA, sB, 0, kA, vA, vB, LDKV(vB, vv, t1), NOP_);
        STEP(sB, sA, 1, kA, vA, vA, NOP_, NOP_);
        STEP(sA, sB, 2, kA, vA, vA, NOP_, LDKV(kA, ko, t2));
        STEP(sB, sA, 3, kB, vA, vA, NOP_, NOP_);
        STEP(sA, sB, 0, kB, vB, vA, LDKV(vA, vv, t2), NOP_);
        STEP(sB, sA, 1, kB, vB, vB, NOP_, NOP_);
        STEP(sA, sB, 2, kB, vB, vB, NOP_, LDKV(kB, ko, t3));
        STEP(sB, sA, 3, kA, vB, vB, NOP_, NOP_);
    }
    O[3] = MFMA32(vB.f1, BC(p1), O[3]); L[3] = MFMA16(sel, BC(p1), L[3]);
    STAMP(1,2);
    constexpr int RS = 1024 + 64;
    f32x16 Of;
#pragma unroll
    for (int i = 0; i < 4; ++i) {
        float* d = red + (((w & 4) + i) * 4 + kq) * RS;
        if (i == kq) Of = O[i];
        else {
#pragma unroll
            for (int k = 0; k < 4; ++k) *(f32x4*)(d + k * 256 + lane * 4) = (f32x4){O[i][4 * k], O[i][4 * k + 1], O[i][4 * k + 2], O[i][4 * k + 3]}; }
        if (lane < 16) { d[1024 + lane] = L[i][0]; d[1040 + lane] = L[i][1]; }
    }
    if (w == 0) wait_ge(hcnt, 23u);
    wait_lgkm0(); bar();
    float ls = 0.f;
#pragma unroll
    for (int i = 0; i < 4; ++i) { const int sq = (kq + i) & 3; const float* sp = red + (w * 4 + sq) * RS;
        if (i) {
#pragma unroll
            for (int k = 0; k < 4; ++k) { const f32x4 v = *(const f32x4*)(sp + k * 256 + lane * 4); Of[4 * k] += v[0]; Of[4 * k + 1] += v[1]; Of[4 * k + 2] += v[2]; Of[4 * k + 3] += v[3]; } }
        ls += sp[1024 + (lane & 31)]; }
    float chk = 0.f;
#pragma unroll
    for (int i = 0; i < 16; ++i) chk = fmaf(Of[i], 0.f, chk);
    const bool bad = !(ls >= 9.094947017729282e-13f && ls <= 1.2676506002282294e30f && chk == 0.f);
    if (__any(bad)) {
        const bf16x8 q0 = ldfrag(rs, vo, qo + kq * 2048), q1 = ldfrag(rs, vo + 1024, qo + kq * 2048);
        const unsigned ka = S_ * D_ * 2 + head * 64 * 2048, va = 2 * S_ * D_ * 2 + head * 64 * 2048;
        float mx = -INFINITY;
        for (int t = 0; t < 64; ++t) { f32x16 sc = MFMA32(ldfrag(rs, vo, ka + t * 2048), q0, Z); sc = MFMA32(ldfrag(rs, vo + 1024, ka + t * 2048), q1, sc);
#pragma unroll
            for (int i = 0; i < 16; ++i) mx = fmaxf(mx, sc[i]); }
        mx = fmaxf(mx, __shfl_xor(mx, 32));
        f32x4 Lq = {0.f, 0.f, 0.f, 0.f}; Of = Z;
        for (int t = 0; t < 64; ++t) { f32x16 sc = MFMA32(ldfrag(rs, vo, ka + t * 2048), q0, Z); sc = MFMA32(ldfrag(rs, vo + 1024, ka + t * 2048), q1, sc);
#pragma unroll
            for (int i = 0; i < 16; ++i) sc[i] = __builtin_amdgcn_exp2f(sc[i] - mx);
            const u32x4 a = {cvtpk(sc[0], sc[1]), cvtpk(sc[2], sc[3]), cvtpk(sc[4], sc[5]), cvtpk(sc[6], sc[7])}, bb = {cvtpk(sc[8], sc[9]), cvtpk(sc[10], sc[11]), cvtpk(sc[12], sc[13]), cvtpk(sc[14], sc[15])};
            Of = MFMA32(ldfrag(rs, vo, va + t * 2048), BC(a), Of); Of = MFMA32(ldfrag(rs, vo + 1024, va + t * 2048), BC(bb), Of); Lq = MFMA16(sel, BC(a), Lq); Lq = MFMA16(sel, BC(bb), Lq); }
        const float l0 = __shfl(Lq[0], lane & 15), l1 = __shfl(Lq[1], lane & 15); ls = (lane & 16) ? l1 : l0;
    }
    const float rl = 1.0f / ls;
    const int qq = lane & 31, mt = (qg * 4 + kq) * 2 + (qq >> 4);
#pragma unroll
    for (int i = 0; i < 4; ++i) {
        u32x2 o = {cvtpk(Of[4 * i] * rl, Of[4 * i + 1] * rl), cvtpk(Of[4 * i + 2] * rl, Of[4 * i + 3] * rl)};
        st8_dev(a2 + ((size_t)(mt * NKC2 + head) * 64 + i * 16 + (qq & 15)) * 8 + 4 * (lane >> 5), o);
    }
}

constexpr int G2_XB = 16 * 1024, G2_CHB = G2_XB + 2 * 4096, G2_NS = 3, G2_NPH = 18;
constexpr int PQB = 2 * 1024;
struct G2Hook {
    static constexpr int JP = 11, NPRO = 0, NX = 0;
    DI void extra(unsigned) {}
    DI void prologue() const {}
    unsigned* sig; unsigned* need; int w; unsigned pv; long long ja, jb;
    DI void first() const { if (w == 0 && lane_id() == 0) signal(sig); }
    DI void pre() { if (w == 0) asm volatile("global_load_dword %0, %1, %2 sc1" : "+v"(pv) : "v"(0u), "s"(need) : "memory"); }
    template <class St> DI void jump(St& S) {
#pragma unroll
        for (int i = 0; i < 6; ++i) S.sp[i] += i < 4 ? ja : jb; }
    DI void post() { if (w == 0) { asm volatile("" : "+v"(pv)); if ((unsigned)__builtin_amdgcn_readfirstlane(pv) < 8u) wait_ge(need, 8u); } }
};
DI void phase3(const u16* a2, const u16* wo, const u16* wd, const u16* x, char* part, unsigned* cnt, u16* out, unsigned lds0, int xcd, int idx, int w) {
    const int sp = xcd >> 1, bm = (xcd & 1) * 4 + (idx >> 3), bn = idx & 7;
    const int wm = w >> 1, wn = w & 1;
    f32x4 acc[4][4];
#pragma unroll
    for (int m = 0; m < 4; ++m)
#pragma unroll
        for (int n = 0; n < 4; ++n) acc[m][n] = (f32x4){0.f, 0.f, 0.f, 0.f};
    {
        const int lane = lane_id();
        const int g = lane >> 4, q = (lane & 15) >> 2, pp = lane & 3, sw = ((q >> 1) & 1) | ((g & 1) << 1);
        const int r = 8 * (w & 3) + (lane >> 3), cp = lane & 7, j = (cp >> 1) ^ wswz(r), half = cp & 1, P = w >> 2;
        const int col = bn * 128 + 64 * P + 16 * j + 8 * half;
        unsigned wrd[4];
#pragma unroll
        for (int n = 0; n < 4; ++n) wrd[n] = lds0 + G2_XB + wn * 4096 + g * 1024 + q * 128 + 32 * (n ^ sw) + 8 * pp;
        const unsigned xrd = lds0 + wm * 4 * 1024 + lane * 16;
        const u16* wsrc = wd + (size_t)sp * 28 * 32 * D_;
        Stage<6, 0, 4> S;
#pragma unroll
        for (int i = 0; i < 4; ++i) { const int f = 2 * w + (i & 1), ch = i >> 1;
            S.sp[i] = (const char*)a2 + ((size_t)(bm * 16 + f) * NKC2 + 32 + sp * 28 + ch) * 1024; S.vo[i] = lane * 16; S.sinc[i] = 2048; S.sl[i] = ch * G2_CHB + f * 1024; }
#pragma unroll
        for (int i = 0; i < 2; ++i) { S.sp[4 + i] = (const char*)(wsrc + (size_t)32 * i * D_); S.vo[4 + i] = (r * D_ + col) * 2; S.sinc[4 + i] = 64 * D_ * 2; S.sl[4 + i] = i * G2_CHB + G2_XB + w * 1024; }
        G2Hook H{cnt + (CNT_O + (idx & 7) * 4 + (xcd >> 1)) * 64, cnt + (CNT_O + bm * 4 + sp) * 64, w, 0u, (long long)(sp * 8 - (32 + sp * 28 + 28)) * 1024,
                 (long long)((const char*)(wo + (size_t)sp * 8 * 32 * D_) - (const char*)(wsrc + (size_t)28 * 32 * D_))};
        gemm_core<4, 4, 0, 2, G2_NS, G2_NPH, G2_CHB>(acc, S, lds0, xrd, wrd, w >= 4, H);
    }
    STAMP(2,2);
    const int lane = lane_id(), c = lane & 15, g = lane >> 4, tile = bm * 8 + bn;
    const __amdgpu_buffer_rsrc_t rs = __builtin_amdgcn_make_buffer_rsrc(part + (size_t)tile * (16 * 8 * PQB), 0, 16 * 8 * PQB, 0x00020000);
    f32x4 own[4];
#pragma unroll
    for (int m = 0; m < 4; ++m) {
        if (m == sp) {
#pragma unroll
            for (int n = 0; n < 4; ++n) own[n] = acc[m][n];
        } else {
            const unsigned o = ((m * 4 + sp) * 8 + w) * PQB + lane * 16;
#pragma unroll
            for (int n = 0; n < 4; n += 2) { const u32x4 v = {cvtpk(acc[m][n][0], acc[m][n][1]), cvtpk(acc[m][n][2], acc[m][n][3]), cvtpk(acc[m][n + 1][0], acc[m][n + 1][1]), cvtpk(acc[m][n + 1][2], acc[m][n + 1][3])};
                __builtin_amdgcn_raw_buffer_store_b128(v, rs, o + n * 512, 0, 16); }
        }
    }
    if (w < 4) bar();
    const size_t oo = (size_t)(bm * 256 + wm * 64 + 16 * sp + c) * D_ + bn * 128 + wn * 64 + 4 * g;
    u32x2 xv[4];
#pragma unroll
    for (int n = 0; n < 4; ++n) xv[n] = *(const u32x2*)(x + oo + 16 * n);
    asm volatile("s_waitcnt vmcnt(0)" ::: "memory");
    STAMP(2,3);
    bar();
    if (w == 0) { if (lane == 0) signal(cnt + tile * 64); wait_ge(cnt + tile * 64, 4u); }
    bar();
    STAMP(2,4);
#pragma unroll
    for (int k = 1; k < 4; ++k) {
        const unsigned o = ((sp * 4 + ((sp + k) & 3)) * 8 + w) * PQB + lane * 16;
#pragma unroll
        for (int n = 0; n < 4; n += 2) { const u32x4 v = __builtin_amdgcn_raw_buffer_load_b128(rs, o + n * 512, 0, 16);
            own[n][0] += __uint_as_float(v[0] << 16); own[n][1] += __uint_as_float(v[0] & 0xffff0000u); own[n][2] += __uint_as_float(v[1] << 16); own[n][3] += __uint_as_float(v[1] & 0xffff0000u);
            own[n + 1][0] += __uint_as_float(v[2] << 16); own[n + 1][1] += __uint_as_float(v[2] & 0xffff0000u); own[n + 1][2] += __uint_as_float(v[3] << 16); own[n + 1][3] += __uint_as_float(v[3] & 0xffff0000u); }
    }
#pragma unroll
    for (int n = 0; n < 4; ++n) { f32x4 v = own[n];
        v[0] += __uint_as_float(xv[n][0] << 16); v[1] += __uint_as_float(xv[n][0] & 0xffff0000u); v[2] += __uint_as_float(xv[n][1] << 16); v[3] += __uint_as_float(xv[n][1] & 0xffff0000u);
        u32x2 ov = {cvtpk(v[0], v[1]), cvtpk(v[2], v[3])}; *(u32x2*)(out + oo + 16 * n) = ov; }
}

__global__ __launch_bounds__(512, 2) void mega_kernel(const u16* __restrict__ x, const u16* __restrict__ wqkv, const u16* __restrict__ wo, const u16* __restrict__ wg, const u16* __restrict__ wu,
                                                     const u16* __restrict__ wd, char* ws, u16* __restrict__ out) {
    __shared__ __attribute__((aligned(16))) char smem_[G1_NS * G1_CHB + 8192];
    LAS char* smem = (LAS char*)smem_;
    const unsigned lds0 = (unsigned)(uintptr_t)smem;
    const int w = __builtin_amdgcn_readfirstlane(threadIdx.x >> 6);
    const int xcd = blockIdx.x & 7, idx = blockIdx.x >> 3;
    u16* Qf = (u16*)(ws + WS_Q); u16* a2 = (u16*)(ws + WS_A2); unsigned* cnt = (unsigned*)(ws + WS_CNT);
    STAMP(0,0); STAMPR(0,8);
    const int bn = xcd * 4 + (idx >> 3);
    phase1((const u16*)(ws + WS_XN), wqkv, wg, wu, wo, wd, Qf, (u16*)(ws + WS_K), (u16*)(ws + WS_V), a2, smem, lds0, idx & 7, bn, w);
    STAMP(0,3);
    asm volatile("s_waitcnt vmcnt(0)" ::: "memory");
    STAMP(0,4); STAMPR(0,9);
    bar();
    STAMP(3,0); STAMPR(3,8);
    if (w == 0) {
        const int l = lane_id(), pr = bn * 10 + l;
        if (l < 10 && pr < 96) signal(cnt + (CNT_QKV + (pr & 31)) * 64);
        if (l == 10 && bn >= 9) signal(cnt + (CNT_H + (idx & 7)) * 64);
        wait_ge(cnt + (CNT_QKV + bn) * 64, 24u);
    }
    bar();
    STAMP(3,1); STAMP(3,2); STAMP(3,3); STAMP(3,4); STAMPR(3,9);
    STAMP(1,0); STAMPR(1,8);
    phase2(Qf, a2, cnt + (CNT_H + (xcd & 1) * 4 + (idx >> 3)) * 64, (float*)smem_, bn, idx & 7, w, wo, wd, xcd, idx);
    STAMP(1,3);
    wait_lgkm0(); bar();
    STAMP(1,4); STAMPR(1,9);
    STAMP(2,0); STAMPR(2,8);
    phase3(a2, wo, wd, x, ws + WS_PART, cnt, out, lds0, xcd, idx, w);
}

extern "C" void kernel_launch(void* const* d_in, const int* in_sizes, int n_in, void* d_out, int out_size, void* d_ws, size_t ws_size, hipStream_t stream) {
    const u16* x = (const u16*)d_in[0]; const u16* wqkv = (const u16*)d_in[1]; const u16* wo = (const u16*)d_in[2];
    const u16* wg = (const u16*)d_in[4]; const u16* wu = (const u16*)d_in[5]; const u16* wd = (const u16*)d_in[6];
    char* ws = (char*)d_ws;
    ln_kernel<<<S_ / 4, 256, 0, stream>>>(x, (u16*)(ws + WS_XN), (unsigned*)(ws + WS_CNT), wqkv, wg, wu);
    mega_kernel<<<256, 512, 0, stream>>>(x, wqkv, wo, wg, wu, wd, ws, (u16*)d_out);
}
```

```cpp
#include <hip/hip_runtime.h>
#include <stdint.h>
#include <type_traits>

typedef unsigned short u16;
typedef short bf16x8 __attribute__((ext_vector_type(8)));
typedef short s16x4 __attribute__((ext_vector_type(4)));
typedef float f32x2 __attribute__((ext_vector_type(2)));
typedef float f32x4 __attribute__((ext_vector_type(4)));
typedef float f32x16 __attribute__((ext_vector_type(16)));
typedef unsigned u32x2 __attribute__((ext_vector_type(2)));
typedef unsigned u32x4 __attribute__((ext_vector_type(4)));
typedef __bf16 bf16x2_t __attribute__((ext_vector_type(2)));
#define LAS __attribute__((address_space(3)))
#define DI __device__ __forceinline__

constexpr int LN_PF = 2;
constexpr int S_ = 2048, D_ = 1024, F_ = 3584;
constexpr int NKC1 = D_ / 32;
constexpr int K2 = D_ + F_;
constexpr int NKC2 = K2 / 32;

constexpr size_t WS_XN = 0;
constexpr size_t WS_Q = WS_XN + (size_t)S_ * D_ * 2;
constexpr size_t WS_K = WS_Q + (size_t)S_ * D_ * 2;
constexpr size_t WS_V = WS_K + (size_t)S_ * D_ * 2;
constexpr size_t WS_A2 = WS_V + (size_t)S_ * D_ * 2;
constexpr size_t WS_CNT = WS_A2 + (size_t)S_ * K2 * 2;
constexpr size_t WS_PART = WS_CNT + 65536;

DI unsigned cvtpk(float lo, float hi) { f32x2 v = {lo, hi}; bf16x2_t b = __builtin_convertvector(v, bf16x2_t); return __builtin_bit_cast(unsigned, b); }
DI float rbf(float v) { return __uint_as_float(cvtpk(v, v) << 16); }
DI void glds16(const void* g, unsigned lds_dst_) {
    unsigned keep; const unsigned lds_dst = __builtin_amdgcn_readfirstlane(lds_dst_);
    asm volatile("s_mov_b32 %0, m0\n\ts_mov_b32 m0, %2\n\ts_nop 0\n\tglobal_load_lds_dwordx4 %1, off\n\ts_mov_b32 m0, %0" : "=&s"(keep) : "v"(g), "s"(lds_dst) : "memory");
}
DI void glds16s(const void* sbase, unsigned voff, unsigned lds_dst_) {
    unsigned keep; const unsigned lds_dst = __builtin_amdgcn_readfirstlane(lds_dst_);
    asm volatile("s_mov_b32 %0, m0\n\ts_mov_b32 m0, %2\n\ts_nop 0\n\tglobal_load_lds_dwordx4 %1, %3\n\ts_mov_b32 m0, %0" : "=&s"(keep) : "v"(voff), "s"(lds_dst), "s"(sbase) : "memory");
}
DI void glds16s_dev(const void* sbase, unsigned voff, unsigned lds_dst_) {
    unsigned keep; const unsigned lds_dst = __builtin_amdgcn_readfirstlane(lds_dst_);
    asm volatile("s_mov_b32 %0, m0\n\ts_mov_b32 m0, %2\n\ts_nop 0\n\tglobal_load_lds_dwordx4 %1, %3 sc1\n\ts_mov_b32 m0, %0" : "=&s"(keep) : "v"(voff), "s"(lds_dst), "s"(sbase) : "memory");
}
template <int N> DI void wait_vm() { asm volatile("s_waitcnt vmcnt(%0)" ::"n"(N) : "memory"); }
DI void wait_lgkm0() { asm volatile("s_waitcnt lgkmcnt(0)" ::: "memory"); }
#define SB() __builtin_amdgcn_sched_barrier(0)
#define PIN(x) asm volatile("" : "+v"(x))
DI void bar() { SB(); asm volatile("s_barrier" ::: "memory"); SB(); }
DI int lane_id() { int l; asm volatile("v_mbcnt_lo_u32_b32 %0, -1, 0\n\tv_mbcnt_hi_u32_b32 %0, -1, %0" : "=v"(l)); return l; }
DI s16x4 trread(const LAS char* p) { return __builtin_bit_cast(s16x4, __builtin_amdgcn_ds_read_tr16_b64_v4i16((LAS s16x4*)p)); }
#define MFMA32(a, b, c) __builtin_amdgcn_mfma_f32_32x32x16_bf16(a, b, c, 0, 0, 0)
#define MFMA16(a, b, c) __builtin_amdgcn_mfma_f32_16x16x32_bf16(a, b, c, 0, 0, 0)

template <int CTRL> DI float dpp_add(float v) { return v + __builtin_bit_cast(float, __builtin_amdgcn_update_dpp(0, __builtin_bit_cast(int, v), CTRL, 0xf, 0xf, true)); }
DI float wave_sum(float v) {
    v = dpp_add<0x128>(v); v = dpp_add<0x124>(v); v = dpp_add<0x122>(v); v = dpp_add<0x121>(v);
    auto a = __builtin_amdgcn_permlane16_swap(__float_as_uint(v), __float_as_uint(v), false, false); v = __uint_as_float(a[0]) + __uint_as_float(a[1]);
    auto b = __builtin_amdgcn_permlane32_swap(__float_as_uint(v), __float_as_uint(v), false, false); return __uint_as_float(b[0]) + __uint_as_float(b[1]);
}
__global__ __launch_bounds__(256) void ln_kernel(const u16* __restrict__ x, u16* __restrict__ xn, unsigned* __restrict__ cnt, const u16* __restrict__ wqkv, const u16* __restrict__ wg, const u16* __restrict__ wu) {
    const int lane = threadIdx.x & 63, w = threadIdx.x >> 6;
    const int row = blockIdx.x * 4 + w;
    const u32x4* src = (const u32x4*)(x + (size_t)row * D_ + lane * 16);
    u32x4 a, b;
    asm volatile("global_load_dwordx4 %0, %2, off\n\tglobal_load_dwordx4 %1, %2, off offset:16" : "=&v"(a), "=&v"(b) : "v"(src) : "memory");
    if (blockIdx.x == 0 && threadIdx.x < 136  ) cnt[threadIdx.x * 64] = 0u;
    unsigned pf = 0;
    {
        constexpr unsigned NG = LN_PF * 64 * 56, NQ = LN_PF * 64 * 48; static_assert(5 * LN_PF <= 64 && 2 * NG + NQ == 2048 * 5 * LN_PF, "");
        if (lane < 5 * LN_PF) { const unsigned t = row * (5 * LN_PF) + lane;
            const char* p = t < NG ? (const char*)wg + (size_t)t * 128 : t < 2 * NG ? (const char*)wu + (size_t)(t - NG) * 128 : (const char*)wqkv + (size_t)(t - 2 * NG) * 128;
            asm volatile("global_load_dword %0, %1, off" : "+v"(pf) : "v"(p) : "memory"); }
        asm volatile("s_waitcnt vmcnt(1)" : "+v"(a), "+v"(b) :: "memory");
    }
    float v[16];
#pragma unroll
    for (int i = 0; i < 4; ++i) { v[2 * i] = __uint_as_float(a[i] << 16); v[2 * i + 1] = __uint_as_float(a[i] & 0xffff0000u);
                                  v[8 + 2 * i] = __uint_as_float(b[i] << 16); v[8 + 2 * i + 1] = __uint_as_float(b[i] & 0xffff0000u); }
    float s = 0.f;
#pragma unroll
    for (int i = 0; i < 16; ++i) s += v[i];
    const float mean = wave_sum(s) * (1.0f / D_);
    float q = 0.f;
#pragma unroll
    for (int i = 0; i < 16; ++i) { const float d = v[i] - mean; q += d * d; }
    const float mb = rbf(mean), rs = 1.0f / rbf(sqrtf(rbf(rbf(wave_sum(q) * (1.0f / D_)) + 1e-5f)));
#pragma unroll
    for (int i = 0; i < 16; ++i) v[i] = rbf(v[i] - mb);
    u32x4 w0 = {cvtpk(v[0] * rs, v[1] * rs), cvtpk(v[2] * rs, v[3] * rs), cvtpk(v[4] * rs, v[5] * rs), cvtpk(v[6] * rs, v[7] * rs)};
    u32x4 w1 = {cvtpk(v[8] * rs, v[9] * rs), cvtpk(v[10] * rs, v[11] * rs), cvtpk(v[12] * rs, v[13] * rs), cvtpk(v[14] * rs, v[15] * rs)};
    const int mt = row >> 4, c = row & 15, kc = lane >> 1, g = (lane & 1) * 2;
    u32x4* dst = (u32x4*)xn + ((size_t)(mt * NKC1 + kc) * 64 + g * 16 + c);
    dst[0] = w0; dst[16] = w1;
    asm volatile("s_waitcnt vmcnt(0)" : "+v"(pf) :: "memory");
}

template <int NLS, int NLV, int NDEV = 0> struct Stage { const char* sp[NLS]; unsigned vo[NLS], sinc[NLS], sl[NLS]; const char* gp[NLV ? NLV : 1]; unsigned ginc[NLV ? NLV : 1], gl[NLV ? NLV : 1]; };
template <int NLS, int NLV, int NDEV> DI void stage_issue(const Stage<NLS, NLV, NDEV>& S, int i, unsigned base) {
    if (i < NDEV) glds16s_dev(S.sp[i], S.vo[i], base + S.sl[i]); else if (i < NLS) glds16s(S.sp[i], S.vo[i], base + S.sl[i]); else glds16(S.gp[i - NLS], base + S.gl[i - NLS]); }
struct NoHook { static constexpr bool PRE0 = false; static constexpr int JP = -1, NPRO = 0, NX = 0; DI void extra(unsigned) {} DI void prologue() const {} DI void first() const {} DI void pre() {} DI void post() {} template <class St> DI void jump(St&) {} };
template <bool B> using BoolC = std::integral_constant<bool, B>;
template <int N> using IntC = std::integral_constant<int, N>;
template <int MT, int NT, int N0, int CPP, int NS, int NPH, int CHB, int NLS, int NLV, int NDEV, class Hook>
DI void gemm_core(f32x4 (&acc)[MT][NT], Stage<NLS, NLV, NDEV>& S, const unsigned lds0, unsigned xrd, unsigned (&wrd)[4], const bool trailing, Hook& H) {
    constexpr int SLOTB = CPP * CHB, NLD = NLS + NLV, NRD = CPP * (MT + NT);
    static_assert(NS == 3 || NS == 4, "ring depth");
    auto pro = [&](int p) {
#pragma unroll
        for (int i = 0; i < NLD; ++i) stage_issue(S, i, lds0 + p * SLOTB);
#pragma unroll
        for (int i = 0; i < NLS; ++i) S.sp[i] += S.sinc[i];
#pragma unroll
        for (int i = 0; i < NLV; ++i) S.gp[i] += S.ginc[i];
    };
    if (!Hook::PRE0) pro(0);
#pragma unroll
    for (int m = 0; m < MT; ++m)
#pragma unroll
        for (int n = 0; n < NT; ++n) PIN(acc[m][n]);
    pro(1);
    wait_vm<NLD>();
    bar();
    H.first();
    if (NS == 4) pro(2);
    if (trailing) bar();

    int slot = 0, pslot = (NS - 1) * SLOTB;
    auto phase = [&](auto issue_c, auto wait_c, auto hook_c, auto extra_c) {
        constexpr bool ISSUE = decltype(issue_c)::value, HOOK = decltype(hook_c)::value, EXTRA = decltype(extra_c)::value; constexpr int WAITN = decltype(wait_c)::value;
        bf16x8 xf[CPP][MT], wf[CPP][NT];
        if (HOOK) H.pre();
#pragma unroll
        for (int r = 0; r < NRD; ++r) {
            if (ISSUE) {
#pragma unroll
                for (int i = 0; i < NLD; ++i) if (r == (i * NRD) / NLD + 1) stage_issue(S, i, lds0 + pslot);
            }
            const int c = r / (MT + NT), k = r % (MT + NT);
            if (k < MT) xf[c][k] = *(const LAS bf16x8*)(uintptr_t)(xrd + c * CHB + k * 1024);
            else { const int n = k - MT; const LAS char* p = (const LAS char*)(uintptr_t)(wrd[(N0 + n) & 3] + c * CHB + ((N0 + n) >> 2) * 4096);
                const s16x4 lo = trread(p), hi = trread(p + 512); wf[c][n] = __builtin_shufflevector(lo, hi, 0, 1, 2, 3, 4, 5, 6, 7); }
        }
        if (EXTRA) H.extra(S.vo[0]);
        pslot = slot; slot += SLOTB; int dl = SLOTB; if (slot == NS * SLOTB) { slot = 0; dl = -(NS - 1) * SLOTB; }
        SB();
#pragma unroll
        for (int i = 0; i < 4; ++i) { wrd[i] += dl; PIN(wrd[i]); }
        xrd += dl; PIN(xrd);
        if (ISSUE) {
#pragma unroll
            for (int i = 0; i < NLS; ++i) S.sp[i] += S.sinc[i];
#pragma unroll
            for (int i = 0; i < NLV; ++i) { S.gp[i] += S.ginc[i]; PIN(S.gp[i]); }
        }
        if (HOOK) H.jump(S);
        SB();
        wait_vm<WAITN>();
        if (HOOK) H.post();
        wait_lgkm0();
        bar();
        __builtin_amdgcn_s_setprio(1);
#pragma unroll
        for (int c = 0; c < CPP; ++c)
#pragma unroll
            for (int m = 0; m < MT; ++m)
#pragma unroll
                for (int n = 0; n < NT; ++n) acc[m][n] = MFMA16(wf[c][n], xf[c][m], acc[m][n]);
        __builtin_amdgcn_s_setprio(0);
        bar();
    };
    constexpr int NI = NPH - (NS - 1), JP = Hook::JP, NX = Hook::NX;
    constexpr int WN = (NS - 2) * NLD, WX = WN + (NX ? 1 : 0);
    if constexpr (JP >= 0) {
#pragma unroll 1
        for (int j = 0; j < JP; ++j) phase(BoolC<true>{}, IntC<WX>{}, BoolC<false>{}, BoolC<(NX > 0)>{});
        phase(BoolC<true>{}, IntC<WX>{}, BoolC<true>{}, BoolC<(NX > 0)>{});
    }
    if constexpr (NX > 0) {
#pragma unroll 1
        for (int j = JP + 1; j < NX; ++j) phase(BoolC<true>{}, IntC<WX>{}, BoolC<false>{}, BoolC<true>{});
    }
#pragma unroll 1
    for (int j = (NX > 0 ? NX : JP + 1); j < NI; ++j) phase(BoolC<true>{}, IntC<WN>{}, BoolC<false>{}, BoolC<false>{});
    if (NS == 4) phase(BoolC<false>{}, IntC<NLD>{}, BoolC<false>{}, BoolC<false>{});
#pragma unroll 1
    for (int j = 0; j < 2; ++j) phase(BoolC<false>{}, IntC<0>{}, BoolC<false>{}, BoolC<false>{});
}
DI int wswz(int r) { return ((r >> 1) & 1) | (((r >> 3) & 1) << 1); }

typedef __attribute__((address_space(1))) unsigned gu32;
#define RLX_AGENT __ATOMIC_RELAXED, __HIP_MEMORY_SCOPE_AGENT
DI void st16_dev(u32x4 v, __amdgpu_buffer_rsrc_t rs, unsigned off) { __builtin_amdgcn_raw_buffer_store_b128(v, rs, off, 0, 16); }
DI void signal(unsigned* c) { __hip_atomic_fetch_add((gu32*)c, 1u, RLX_AGENT); }
DI void wait_ge(unsigned* c, unsigned n) {
#pragma unroll 1
    for (unsigned spins = 0; spins < (1u << 22); ++spins) {
        if ((unsigned)__builtin_amdgcn_readfirstlane(__hip_atomic_load((gu32*)c, RLX_AGENT)) >= n) break;
        __builtin_amdgcn_s_sleep(1);
    }
}
constexpr int CNT_QKV = 64, CNT_H = 96, CNT_O = 104, CNT_N = 136;

constexpr int G1_XB = 16 * 1024, G1_CHB = G1_XB + 5 * 4096, G1_NS = 4;
constexpr float QSCALE = 0.17677669529663687f * 1.4426950408889634f;
template <int GRP> struct G1Hook : NoHook {
    static constexpr int NX = GRP == 1 ? 29 : 0;
    const char* xp; unsigned xinc, dummy; int left;
    DI void init(const u16* wqkv, const u16* wg, const u16* wu, const u16* wo, const u16* wd, unsigned gw, unsigned dummy_) {
        constexpr int C0 = LN_PF * 2;
        dummy = dummy_;
        if (gw < 224) { xp = (const char*)wg + (size_t)C0 * 224 * 1024 + gw * 1024; xinc = 224 * 1024; left = 32 - C0; }
        else if (gw < 448) { xp = (const char*)wu + (size_t)C0 * 224 * 1024 + (gw - 224) * 1024; xinc = 224 * 1024; left = 32 - C0; }
        else if (gw < 640) { xp = (const char*)wqkv + (size_t)C0 * 192 * 1024 + (gw - 448) * 1024; xinc = 192 * 1024; left = 32 - C0; }
        else if (gw < 896) { xp = (const char*)wd + (gw - 640) * 1024; xinc = 256 * 1024; left = 28; }
        else { xp = (const char*)wo + (gw - 896) * 1024; xinc = 128 * 1024; left = 16; }
    }
    DI void extra(unsigned vo) { glds16s(xp, vo, dummy); --left; xp += left > 0 ? xinc : 0u; }
};
template <int GRP>
DI void gemm1_body(f32x4 (&acc)[4][10], const u16* xn, const u16* wqkv, const u16* wg, const u16* wu, const u16* wo, const u16* wd, unsigned lds0, int bm, int bn, int w) {
    constexpr int NLV = GRP == 0 ? 3 : 2;
    const int lane = lane_id();
    Stage<2, NLV> S;
#pragma unroll
    for (int i = 0; i < 2; ++i) { const int f = 2 * w + i; S.sp[i] = (const char*)xn + ((size_t)(bm * 16 + f) * NKC1) * 1024; S.vo[i] = lane * 16; S.sinc[i] = 1024; S.sl[i] = f * 1024; }
#pragma unroll
    for (int i = 0; i < NLV; ++i) {
        const int b = w + 8 * i, P = bn * 5 + (b >> 2), r = 8 * (b & 3) + (lane >> 3), cp = lane & 7, j = (cp >> 1) ^ wswz(r), half = cp & 1;
        if (P < 48) { S.gp[i] = (const char*)(wqkv + (size_t)r * 3072 + 64 * P + 16 * j + 8 * half); S.ginc[i] = 32 * 3072 * 2; }
        else { S.gp[i] = (const char*)(((j & 1) ? wu : wg) + (size_t)r * F_ + 32 * (P - 48) + 16 * (j >> 1) + 8 * half); S.ginc[i] = 32 * F_ * 2; }
        S.gl[i] = G1_XB + b * 1024;
    }
    const int g = lane >> 4, q = (lane & 15) >> 2, pp = lane & 3, sw = ((q >> 1) & 1) | ((g & 1) << 1);
    unsigned wrd[4];
#pragma unroll
    for (int j = 0; j < 4; ++j) wrd[j] = lds0 + G1_XB + g * 1024 + q * 128 + 32 * (j ^ sw) + 8 * pp;
    const unsigned xrd = lds0 + (w & 3) * 4 * 1024 + lane * 16;
    G1Hook<GRP> H; H.init(wqkv, wg, wu, wo, wd, blockIdx.x * 4 + (w & 3), lds0 + G1_NS * G1_CHB + (w & 3) * 1024);
    gemm_core<4, 10, 10 * GRP, 1, G1_NS, NKC1, G1_CHB>(acc, S, lds0, xrd, wrd, GRP == 1, H);
}
DI void phase1(const u16* xn, const u16* wqkv, const u16* wg, const u16* wu, const u16* wo, const u16* wd, u16* Qf, LAS char* smem, unsigned lds0, int bm, int bn, int w) {
    f32x4 acc[4][10];
#pragma unroll
    for (int m = 0; m < 4; ++m)
#pragma unroll
        for (int n = 0; n < 10; ++n) acc[m][n] = (f32x4){0.f, 0.f, 0.f, 0.f};
    if (w < 4) gemm1_body<0>(acc, xn, wqkv, wg, wu, wo, wd, lds0, bm, bn, w);
    else gemm1_body<1>(acc, xn, wqkv, wg, wu, wo, wd, lds0, bm, bn, w);

    const int lane2 = lane_id();
    const int wm = w & 3, wn = w >> 2, c = lane2 & 15, g = lane2 >> 4;
    const int mt0 = bm * 16 + wm * 4;
    const __amdgpu_buffer_rsrc_t rq = __builtin_amdgcn_make_buffer_rsrc((void*)Qf, 0, 3 * S_ * D_ * 2 + S_ * K2 * 2, 0x00020000);
    const unsigned vqk = ((g & 1) * 64 + c + 32 * (g >> 1)) * 16, vv = ((g >> 1) * 16 + c + 32 * (g & 1)) * 16, vh = ((g & 1) * NKC2 * 64 + (g >> 1) * 16 + c) * 16;
#pragma unroll
    for (int i = 0; i < 5; ++i) {
        if (i == 1 && w < 4) bar();
        const int pr = bn * 10 + wn * 5 + i;
        if (pr < 64) {
            const float sc = pr < 32 ? QSCALE : 1.0f;
            const unsigned so = (unsigned)(pr * 64 + (mt0 >> 1)) * 2048;
#pragma unroll
            for (int m = 0; m < 4; ++m) { const f32x4 a = acc[m][2 * i] * sc, b = acc[m][2 * i + 1] * sc;
                const auto s0 = __builtin_amdgcn_permlane16_swap(cvtpk(a[0], a[1]), cvtpk(b[0], b[1]), false, false), s1 = __builtin_amdgcn_permlane16_swap(cvtpk(a[2], a[3]), cvtpk(b[2], b[3]), false, false);
                st16_dev((u32x4){s0[0], s1[0], s0[1], s1[1]}, rq, vqk + so + (m >> 1) * 2048 + (m & 1) * 256); }
        } else if (pr < 96) {
            LAS char* tb = smem + w * 4096;
#pragma unroll
            for (int m = 0; m < 4; ++m)
#pragma unroll
                for (int dh = 0; dh < 2; ++dh) { const f32x4 v = acc[m][2 * i + dh]; u32x2 o = {cvtpk(v[0], v[1]), cvtpk(v[2], v[3])};
                    *(LAS u32x2*)(tb + (m * 2 + dh) * 512 + c * 32 + g * 8) = o; }
            const unsigned so = (unsigned)(pr * 64 + (mt0 >> 1)) * 2048;
            const LAS char* tr = tb + (g >> 1) * 512 + (4 * (g & 1) + (c >> 2)) * 32 + (c & 3) * 8;
#pragma unroll
            for (int m = 0; m < 4; ++m) { const u32x2 lo = __builtin_bit_cast(u32x2, trread(tr + m * 1024)), hi = __builtin_bit_cast(u32x2, trread(tr + m * 1024 + 256));
                st16_dev((u32x4){lo[0], lo[1], hi[0], hi[1]}, rq, vv + so + m * 1024); }
        } else {
            const int hp = pr - 96;
            const unsigned so = (unsigned)(3 * S_ * D_ * 2) + (unsigned)((mt0 * NKC2 + 32 + (hp >> 1)) * 64 + (hp & 1) * 32) * 16;
#pragma unroll
            for (int m = 0; m < 4; m += 2) { unsigned pk[2][2];
#pragma unroll
                for (int k = 0; k < 2; ++k) { const f32x4 gt = acc[m + k][2 * i], up = acc[m + k][2 * i + 1]; f32x4 hv;
#pragma unroll
                    for (int r = 0; r < 4; ++r) hv[r] = gt[r] * up[r] * __builtin_amdgcn_rcpf(1.0f + __builtin_amdgcn_exp2f(-1.4426950408889634f * gt[r]));
                    pk[k][0] = cvtpk(hv[0], hv[1]); pk[k][1] = cvtpk(hv[2], hv[3]); }
                const auto s0 = __builtin_amdgcn_permlane16_swap(pk[0][0], pk[1][0], false, false), s1 = __builtin_amdgcn_permlane16_swap(pk[0][1], pk[1][1], false, false);
                st16_dev((u32x4){s0[0], s1[0], s0[1], s1[1]}, rq, vh + so + m * NKC2 * 1024); }
        }
    }
}

constexpr int PQB = 2 * 1024;
constexpr unsigned EMPTY = 0xFFFFFFFFu;
DI void arm_inbox(char* part, int xcd, int idx, int w) {
    const int sp = xcd >> 1, tile = ((xcd & 1) * 4 + (idx >> 3)) * 8 + (idx & 7);
    const __amdgpu_buffer_rsrc_t rs = __builtin_amdgcn_make_buffer_rsrc(part + (size_t)tile * (16 * 8 * PQB), 0, 16 * 8 * PQB, 0x00020000);
    const unsigned vo = lane_id() * 16;
#pragma unroll
    for (int k = 1; k < 4; ++k)
#pragma unroll
        for (int n = 0; n < 2; ++n) st16_dev((u32x4){EMPTY, EMPTY, EMPTY, EMPTY}, rs, vo + n * 1024 + ((sp * 4 + ((sp + k) & 3)) * 8 + w) * PQB);
}

constexpr int G2_XB = 16 * 1024, G2_CHB = G2_XB + 2 * 4096, G2_NS = 3, G2_NPH = 18;
DI void p3_stage(Stage<6, 0, 4>& S, const u16* a2, const u16* wd, unsigned lds0, int xcd, int idx, int w) {
    const int sp = xcd >> 1, bm = (xcd & 1) * 4 + (idx >> 3), bn = idx & 7, lane = lane_id();
    const int r = 8 * (w & 3) + (lane >> 3), cp = lane & 7, j = (cp >> 1) ^ wswz(r), half = cp & 1, P = w >> 2;
    const int col = bn * 128 + 64 * P + 16 * j + 8 * half;
    const u16* wsrc = wd + (size_t)sp * 28 * 32 * D_;
#pragma unroll
    for (int i = 0; i < 4; ++i) { const int f = 2 * w + (i & 1), ch = i >> 1;
        S.sp[i] = (const char*)a2 + ((size_t)(bm * 16 + f) * NKC2 + 32 + sp * 28 + ch) * 1024; S.vo[i] = lane * 16; S.sinc[i] = 2048; S.sl[i] = ch * G2_CHB + f * 1024; }
#pragma unroll
    for (int i = 0; i < 2; ++i) { S.sp[4 + i] = (const char*)(wsrc + (size_t)32 * i * D_); S.vo[4 + i] = (r * D_ + col) * 2; S.sinc[4 + i] = 64 * D_ * 2; S.sl[4 + i] = i * G2_CHB + G2_XB + w * 1024; }
#pragma unroll
    for (int i = 0; i < 6; ++i) { stage_issue(S, i, lds0); S.sp[i] += S.sinc[i]; }
}

struct KV { bf16x8 f0, f1; };
DI bf16x8 ldfrag(__amdgpu_buffer_rsrc_t rs, unsigned voff, unsigned soff) { return __builtin_bit_cast(bf16x8, __builtin_amdgcn_raw_buffer_load_b128(rs, voff, soff, 16)); }
DI void phase2(const u16* Qf, u16* a2, unsigned* hcnt, float* red, int head, int qb, int w, char* part, int xcd, int idx, Stage<6, 0, 4>& S3, const u16* wd, unsigned lds0) {
    const int lane = lane_id();
    const int kq = w & 3, qg = qb * 2 + (w >> 2);
    const __amdgpu_buffer_rsrc_t rs = __builtin_amdgcn_make_buffer_rsrc((void*)Qf, 0, 3 * S_ * D_ * 2, 0x00020000);
    const unsigned vo = lane * 16;
    const unsigned ko = S_ * D_ * 2 + (head * 64 + kq * 16) * 2048, vv = 2 * S_ * D_ * 2 + (head * 64 + kq * 16) * 2048, qo = (head * 64 + qg * 4) * 2048;
    KV kA, kB, vA, vB; bf16x8 q[4][2];
    kA.f0 = ldfrag(rs, vo, ko); kA.f1 = ldfrag(rs, vo + 1024, ko);
#pragma unroll
    for (int i = 0; i < 4; ++i) { q[i][0] = ldfrag(rs, vo, qo + i * 2048); q[i][1] = ldfrag(rs, vo + 1024, qo + i * 2048); }
    vA.f0 = ldfrag(rs, vo, vv); vA.f1 = ldfrag(rs, vo + 1024, vv); kB.f0 = ldfrag(rs, vo, ko + 2048); kB.f1 = ldfrag(rs, vo + 1024, ko + 2048);
    vB.f0 = (bf16x8){0, 0, 0, 0, 0, 0, 0, 0}; vB.f1 = vB.f0;
    bf16x8 sel;
    { const short one = ((lane & 15) == ((lane >> 4) & 1)) ? (short)0x3F80 : (short)0;
#pragma unroll
      for (int i = 0; i < 8; ++i) sel[i] = one; }
    f32x16 O[4], sA, sB, Z; f32x4 L[4];
#pragma unroll
    for (int i = 0; i < 16; ++i) { Z[i] = 0.f; O[0][i] = 0.f; O[1][i] = 0.f; O[2][i] = 0.f; O[3][i] = 0.f; }
#pragma unroll
    for (int i = 0; i < 4; ++i) L[i] = (f32x4){0.f, 0.f, 0.f, 0.f};
    u32x4 p0, p1 = {0u, 0u, 0u, 0u};
    sA = MFMA32(kA.f0, q[0][0], Z); sA = MFMA32(kA.f1, q[0][1], sA);

#define EX(x) x = __builtin_amdgcn_exp2f(x)
#define BC(x) __builtin_bit_cast(bf16x8, x)
#define STEP(C, N, QI, KN, VC, VP, LD1, LD2) do { constexpr int NQ = ((QI) + 1) & 3, PQ = ((QI) + 3) & 3; \
    N = MFMA32(KN.f0, q[NQ][0], Z); EX(C[0]); EX(C[1]); EX(C[2]); EX(C[3]); PIN(C); SB(); \
    O[PQ] = MFMA32(VP.f1, BC(p1), O[PQ]); LD1; EX(C[4]); EX(C[5]); EX(C[6]); EX(C[7]); PIN(C); SB(); \
    N = MFMA32(KN.f1, q[NQ][1], N); LD2; \
    p0 = (u32x4){cvtpk(C[0], C[1]), cvtpk(C[2], C[3]), cvtpk(C[4], C[5]), cvtpk(C[6], C[7])}; PIN(p0); EX(C[8]); EX(C[9]); PIN(C); SB(); \
    L[PQ] = MFMA16(sel, BC(p1), L[PQ]); EX(C[10]); EX(C[11]); PIN(C); SB(); \
    O[QI] = MFMA32(VC.f0, BC(p0), O[QI]); EX(C[12]); EX(C[13]); EX(C[14]); EX(C[15]); PIN(C); SB(); \
    L[QI] = MFMA16(sel, BC(p0), L[QI]); p1 = (u32x4){cvtpk(C[8], C[9]), cvtpk(C[10], C[11]), cvtpk(C[12], C[13]), cvtpk(C[14], C[15])}; PIN(p1); SB(); \
    } while (0)
#define NOP_ ((void)0)
#define LDKV(X, base, t) (X.f0 = ldfrag(rs, vo, (base) + (t)), X.f1 = ldfrag(rs, vo + 1024, (base) + (t)))
    for (int t = 0; t < 16; t += 2) {
        const unsigned t1 = (t + 1) * 2048, t2 = ((t + 2) & 15) * 2048, t3 = ((t + 3) & 15) * 2048;
        STEP(sA, sB, 0, kA, vA, vB, LDKV(vB, vv, t1), NOP_);
        STEP(sB, sA, 1, kA, vA, vA, NOP_, NOP_);
        STEP(sA, sB, 2, kA, vA, vA, NOP_, LDKV(kA, ko, t2));
        STEP(sB, sA, 3, kB, vA, vA, NOP_, NOP_);
        STEP(sA, sB, 0, kB, vB, vA, LDKV(vA, vv, t2), NOP_);
        STEP(sB, sA, 1, kB, vB, vB, NOP_, NOP_);
        STEP(sA, sB, 2, kB, vB, vB, NOP_, LDKV(kB, ko, t3));
        STEP(sB, sA, 3, kA, vB, vB, NOP_, NOP_);
    }
    O[3] = MFMA32(vB.f1, BC(p1), O[3]); L[3] = MFMA16(sel, BC(p1), L[3]);
    __builtin_amdgcn_s_waitcnt(0x0F70);

    float* redo = red + 12288; float* redl = red + 36864;
    f32x16 Of;
#pragma unroll
    for (int i = 0; i < 4; ++i) {
        if (i == kq) Of = O[i];
        else { float* d = redo + (((w & 4) + i) * 3 + ((kq - i) & 3) - 1) * 1024;
#pragma unroll
            for (int k = 0; k < 4; ++k) *(f32x4*)(d + k * 256 + lane * 4) = (f32x4){O[i][4 * k], O[i][4 * k + 1], O[i][4 * k + 2], O[i][4 * k + 3]}; }
        if (lane < 16) { float* dl = redl + (((w & 4) + i) * 4 + kq) * 32; dl[lane] = L[i][0]; dl[16 + lane] = L[i][1]; }
    }
    if (w < 4) {
        wait_ge(hcnt, 23u);
        arm_inbox(part, xcd, idx, w); arm_inbox(part, xcd, idx, w + 4);
    }
    wait_lgkm0(); bar();
    p3_stage(S3, a2, wd, lds0, xcd, idx, w);
    float ls = 0.f;
#pragma unroll
    for (int i = 0; i < 4; ++i) {
        if (i) { const float* sp = redo + (w * 3 + i - 1) * 1024;
#pragma unroll
            for (int k = 0; k < 4; ++k) { const f32x4 v = *(const f32x4*)(sp + k * 256 + lane * 4); Of[4 * k] += v[0]; Of[4 * k + 1] += v[1]; Of[4 * k + 2] += v[2]; Of[4 * k + 3] += v[3]; } }
        ls += redl[(w * 4 + i) * 32 + (lane & 31)]; }
    float chk = 0.f;
#pragma unroll
    for (int i = 0; i < 16; ++i) chk = fmaf(Of[i], 0.f, chk);
    const bool bad = !(ls >= 9.094947017729282e-13f && ls <= 1.2676506002282294e30f && chk == 0.f);
    if (__any(bad)) {
        const bf16x8 q0 = ldfrag(rs, vo, qo + kq * 2048), q1 = ldfrag(rs, vo + 1024, qo + kq * 2048);
        const unsigned ka = S_ * D_ * 2 + head * 64 * 2048, va = 2 * S_ * D_ * 2 + head * 64 * 2048;
        float mx = -INFINITY;
        for (int t = 0; t < 64; ++t) { f32x16 sc = MFMA32(ldfrag(rs, vo, ka + t * 2048), q0, Z); sc = MFMA32(ldfrag(rs, vo + 1024, ka + t * 2048), q1, sc);
#pragma unroll
            for (int i = 0; i < 16; ++i) mx = fmaxf(mx, sc[i]); }
        mx = fmaxf(mx, __shfl_xor(mx, 32));
        f32x4 Lq = {0.f, 0.f, 0.f, 0.f}; Of = Z;
        for (int t = 0; t < 64; ++t) { f32x16 sc = MFMA32(ldfrag(rs, vo, ka + t * 2048), q0, Z); sc = MFMA32(ldfrag(rs, vo + 1024, ka + t * 2048), q1, sc);
#pragma unroll
            for (int i = 0; i < 16; ++i) sc[i] = __builtin_amdgcn_exp2f(sc[i] - mx);
            const u32x4 a = {cvtpk(sc[0], sc[1]), cvtpk(sc[2], sc[3]), cvtpk(sc[4], sc[5]), cvtpk(sc[6], sc[7])}, bb = {cvtpk(sc[8], sc[9]), cvtpk(sc[10], sc[11]), cvtpk(sc[12], sc[13]), cvtpk(sc[14], sc[15])};
            Of = MFMA32(ldfrag(rs, vo, va + t * 2048), BC(a), Of); Of = MFMA32(ldfrag(rs, vo + 1024, va + t * 2048), BC(bb), Of); Lq = MFMA16(sel, BC(a), Lq); Lq = MFMA16(sel, BC(bb), Lq); }
        const float l0 = __shfl(Lq[0], lane & 15), l1 = __shfl(Lq[1], lane & 15); ls = (lane & 16) ? l1 : l0;
    }
    const float rl = 1.0f / ls;
    const int qq = lane & 31;
    const __amdgpu_buffer_rsrc_t ra = __builtin_amdgcn_make_buffer_rsrc((void*)a2, 0, S_ * K2 * 2, 0x00020000);
    const unsigned ov = ((qq >> 4) * NKC2 * 64 + (lane >> 5) * 16 + (qq & 15)) * 16, os = (unsigned)(((qg * 4 + kq) * 2 * NKC2 + head) * 1024);
#pragma unroll
    for (int i = 0; i < 4; i += 2) {
        const auto s0 = __builtin_amdgcn_permlane32_swap(cvtpk(Of[4 * i] * rl, Of[4 * i + 1] * rl), cvtpk(Of[4 * i + 4] * rl, Of[4 * i + 5] * rl), false, false);
        const auto s1 = __builtin_amdgcn_permlane32_swap(cvtpk(Of[4 * i + 2] * rl, Of[4 * i + 3] * rl), cvtpk(Of[4 * i + 6] * rl, Of[4 * i + 7] * rl), false, false);
        st16_dev((u32x4){s0[0], s1[0], s0[1], s1[1]}, ra, ov + os + i * 256);
    }
}

struct G2Hook {
    static constexpr bool PRE0 = true;
    static constexpr int JP = 11, NPRO = 0, NX = 0;
    DI void extra(unsigned) {}
    DI void prologue() const {}
    unsigned* sig; unsigned* need; unsigned* need2; int w; unsigned pv, pv2; long long ja, jb;
    DI void first() const { if (w == 4) { const int l = lane_id(); if (l < 2) signal(l ? need2 : sig); } }
    DI void pre() { if (w == 0) asm volatile("global_load_dword %0, %2, %3 sc1\n\tglobal_load_dword %1, %2, %4 sc1" : "+v"(pv), "+v"(pv2) : "v"(0u), "s"(need), "s"(need2) : "memory"); }
    template <class St> DI void jump(St& S) {
#pragma unroll
        for (int i = 0; i < 6; ++i) S.sp[i] += i < 4 ? ja : jb; }
    DI void post() { if (w == 0) { asm volatile("" : "+v"(pv), "+v"(pv2));
        if ((unsigned)__builtin_amdgcn_readfirstlane(pv) < 8u) wait_ge(need, 8u);
        if ((unsigned)__builtin_amdgcn_readfirstlane(pv2) < 4u) wait_ge(need2, 4u); } }
};
DI void phase3(const u16* a2, const u16* wo, const u16* wd, const u16* x, char* part, unsigned* cnt, u16* out, unsigned lds0, int xcd, int idx, int w, Stage<6, 0, 4>& S) {
    const int sp = xcd >> 1, bm = (xcd & 1) * 4 + (idx >> 3), bn = idx & 7;
    const int wm = w >> 1, wn = w & 1;
    f32x4 acc[4][4];
#pragma unroll
    for (int m = 0; m < 4; ++m)
#pragma unroll
        for (int n = 0; n < 4; ++n) acc[m][n] = (f32x4){0.f, 0.f, 0.f, 0.f};
    {
        const int lane = lane_id();
        const int g = lane >> 4, q = (lane & 15) >> 2, pp = lane & 3, sw = ((q >> 1) & 1) | ((g & 1) << 1);
        unsigned wrd[4];
#pragma unroll
        for (int n = 0; n < 4; ++n) wrd[n] = lds0 + G2_XB + wn * 4096 + g * 1024 + q * 128 + 32 * (n ^ sw) + 8 * pp;
        const unsigned xrd = lds0 + wm * 4 * 1024 + lane * 16;
        const u16* wsrc = wd + (size_t)sp * 28 * 32 * D_;
        G2Hook H{cnt + (CNT_O + (idx & 7) * 4 + (xcd >> 1)) * 64, cnt + (CNT_O + bm * 4 + sp) * 64, cnt + (bm * 8 + bn) * 64, w, 0u, 0u, (long long)(sp * 8 - (32 + sp * 28 + 28)) * 1024,
                 (long long)((const char*)(wo + (size_t)sp * 8 * 32 * D_) - (const char*)(wsrc + (size_t)28 * 32 * D_))};
        gemm_core<4, 4, 0, 2, G2_NS, G2_NPH, G2_CHB>(acc, S, lds0, xrd, wrd, w >= 4, H);
    }

    const int lane = lane_id(), c = lane & 15, g = lane >> 4, tile = bm * 8 + bn;
    const __amdgpu_buffer_rsrc_t rs = __builtin_amdgcn_make_buffer_rsrc(part + (size_t)tile * (16 * 8 * PQB), 0, 16 * 8 * PQB, 0x00020000);
    f32x4 own[4];
#pragma unroll
    for (int m = 0; m < 4; ++m) {
        if (m == sp) {
#pragma unroll
            for (int n = 0; n < 4; ++n) own[n] = acc[m][n];
        } else {
            const unsigned o = ((m * 4 + sp) * 8 + w) * PQB;
#pragma unroll
            for (int n = 0; n < 4; n += 2) { u32x4 v = {cvtpk(acc[m][n][0], acc[m][n][1]), cvtpk(acc[m][n][2], acc[m][n][3]), cvtpk(acc[m][n + 1][0], acc[m][n + 1][1]), cvtpk(acc[m][n + 1][2], acc[m][n + 1][3])};
#pragma unroll
                for (int k = 0; k < 4; ++k) v[k] = min(v[k], EMPTY - 1u);
                st16_dev(v, rs, lane * 16 + n * 512 + o); }
        }
    }
    if (w < 4) bar();
    const size_t oo = (size_t)(bm * 256 + wm * 64 + 16 * sp + c) * D_ + bn * 128 + wn * 64 + 4 * g;
    u32x2 xv[4];
#pragma unroll
    for (int n = 0; n < 4; ++n) xv[n] = *(const u32x2*)(x + oo + 16 * n);

    u32x4 pin[3][2];
#pragma unroll 1
    for (unsigned spins = 0; spins < (1u << 20); ++spins) {
        asm volatile("" ::: "memory");
        unsigned mx = 0u;
#pragma unroll
        for (int k = 1; k < 4; ++k)
#pragma unroll
            for (int n = 0; n < 2; ++n) pin[k - 1][n] = __builtin_amdgcn_raw_buffer_load_b128(rs, lane * 16 + n * 1024, ((sp * 4 + ((sp + k) & 3)) * 8 + w) * PQB, 16);
#pragma unroll
        for (int k = 0; k < 3; ++k)
#pragma unroll
            for (int n = 0; n < 2; ++n) mx = max(max(mx, max(pin[k][n][0], pin[k][n][1])), max(pin[k][n][2], pin[k][n][3]));
        if (!__any(mx == EMPTY)) break;
        __builtin_amdgcn_s_sleep(2);
    }

#pragma unroll
    for (int k = 0; k < 3; ++k)
#pragma unroll
        for (int n = 0; n < 4; n += 2) { const u32x4 v = pin[k][n >> 1];
            own[n][0] += __uint_as_float(v[0] << 16); own[n][1] += __uint_as_float(v[0] & 0xffff0000u); own[n][2] += __uint_as_float(v[1] << 16); own[n][3] += __uint_as_float(v[1] & 0xffff0000u);
            own[n + 1][0] += __uint_as_float(v[2] << 16); own[n + 1][1] += __uint_as_float(v[2] & 0xffff0000u); own[n + 1][2] += __uint_as_float(v[3] << 16); own[n + 1][3] += __uint_as_float(v[3] & 0xffff0000u); }
#pragma unroll
    for (int n = 0; n < 4; ++n) { f32x4 v = own[n];
        v[0] += __uint_as_float(xv[n][0] << 16); v[1] += __uint_as_float(xv[n][0] & 0xffff0000u); v[2] += __uint_as_float(xv[n][1] << 16); v[3] += __uint_as_float(xv[n][1] & 0xffff0000u);
        u32x2 ov = {cvtpk(v[0], v[1]), cvtpk(v[2], v[3])}; *(u32x2*)(out + oo + 16 * n) = ov; }
}

__global__ __launch_bounds__(512, 2) void mega_kernel(const u16* __restrict__ x, const u16* __restrict__ wqkv, const u16* __restrict__ wo, const u16* __restrict__ wg, const u16* __restrict__ wu,
                                                     const u16* __restrict__ wd, char* ws, u16* __restrict__ out) {
    __shared__ __attribute__((aligned(16))) char smem_[G1_NS * G1_CHB + 8192];
    LAS char* smem = (LAS char*)smem_;
    const unsigned lds0 = (unsigned)(uintptr_t)smem;
    const int w = __builtin_amdgcn_readfirstlane(threadIdx.x >> 6);
    const int xcd = blockIdx.x & 7, idx = blockIdx.x >> 3;
    u16* Qf = (u16*)(ws + WS_Q); u16* a2 = (u16*)(ws + WS_A2); unsigned* cnt = (unsigned*)(ws + WS_CNT);

    int bn, bm;
    if (idx < 8) { bn = xcd; bm = idx; } else if (idx < 10) { bn = idx; bm = xcd; } else { const int t = 22 * xcd + idx - 10; bn = 10 + (t >> 3); bm = t & 7; }
    const int head = xcd * 4 + (idx >> 3);
    phase1((const u16*)(ws + WS_XN), wqkv, wg, wu, wo, wd, Qf, smem, lds0, bm, bn, w);

    asm volatile("s_waitcnt vmcnt(0)" ::: "memory");

    bar();

    if (w == 0) {
        const int l = lane_id(), pr = bn * 10 + l;
        if (l < 10 && pr < 96) signal(cnt + (CNT_QKV + (pr & 31)) * 64);
        if (l == 10 && bn >= 9) signal(cnt + (CNT_H + bm) * 64);
        wait_ge(cnt + (CNT_QKV + head) * 64, 24u);
    }
    bar();

    Stage<6, 0, 4> S3;
    phase2(Qf, a2, cnt + (CNT_H + (xcd & 1) * 4 + (idx >> 3)) * 64, (float*)smem_, head, idx & 7, w, ws + WS_PART, xcd, idx, S3, wd, lds0);

    wait_lgkm0(); bar();

    phase3(a2, wo, wd, x, ws + WS_PART, cnt, out, lds0, xcd, idx, w, S3);
}

extern "C" void kernel_launch(void* const* d_in, const int* in_sizes, int n_in, void* d_out, int out_size, void* d_ws, size_t ws_size, hipStream_t stream) {
    const u16* x = (const u16*)d_in[0]; const u16* wqkv = (const u16*)d_in[1]; const u16* wo = (const u16*)d_in[2];
    const u16* wg = (const u16*)d_in[4]; const u16* wu = (const u16*)d_in[5]; const u16* wd = (const u16*)d_in[6];
    char* ws = (char*)d_ws;
    ln_kernel<<<S_ / 4, 256, 0, stream>>>(x, (u16*)(ws + WS_XN), (unsigned*)(ws + WS_CNT), wqkv, wg, wu);
    mega_kernel<<<256, 512, 0, stream>>>(x, wqkv, wo, wg, wu, wd, ws, (u16*)d_out);
}
```
